```python
import torch

def _softmax(x, axis=-1):
    return torch.softmax(x, dim=axis)
def _gelu(x):
    return torch.nn.functional.gelu(x, approximate="tanh")
def _silu(x):
    return torch.nn.functional.silu(x)
def _sigmoid(x):
    return torch.sigmoid(x)
def _layernorm(x, axis=-1, eps=1e-5):
    m = x.mean(dim=axis, keepdim=True)
    v = x.var(dim=axis, keepdim=True, unbiased=False)
    return (x - m) / torch.sqrt(v + eps)
def _scatter_add(tgt, idx, src, axis=0):
    return tgt.clone().index_add_(axis, idx, src)
def _segment_sum(src, seg, n):
    out = torch.zeros((n,) + src.shape[1:], dtype=src.dtype)
    return out.index_add_(0, seg, src)
def _pad(x, pw):
    flat = tuple(v for pair in reversed(pw) for v in pair)
    return torch.nn.functional.pad(x, flat)

kernel_name = 'standalone__gqa_softmax__sampling'

def setup_inputs():

    _w = (lambda a: (lambda t: t.to(torch.bfloat16) if t.is_floating_point() else t)(torch.as_tensor(a)))
    B, Hq, Hkv, Sq, Sk, D = 8, 32, 8, 1, 8192, 128
    return {
        "q": _w((lambda *s: torch.randn(*s, dtype=torch.bfloat16) * 0.8)(B, Hq, Sq, D)),
        "k": _w((lambda *s: torch.randn(*s, dtype=torch.bfloat16) * 0.8)(B, Hkv, Sk, D)),
        "v": _w((lambda *s: torch.randn(*s, dtype=torch.bfloat16) * 0.05)(B, Hkv, Sk, D)),
    }

def reference(q, k, v):
    B, Hq, Sq, D = q.shape
    Hkv = k.shape[1]
    rep = Hq // Hkv
    k = (lambda x, n, axis: torch.repeat_interleave(x, n, dim=axis))(k, rep, axis=1)
    v = (lambda x, n, axis: torch.repeat_interleave(x, n, dim=axis))(v, rep, axis=1)
    scale = D ** -0.5
    scores = torch.einsum("bhqd,bhkd->bhqk", q, k) * scale
    p = _softmax(scores, axis=-1)
    return torch.einsum("bhqk,bhkd->bhqd", p, v)
```

```cpp
#include <hip/hip_runtime.h>
#include <hip/hip_bf16.h>
#include <stdint.h>

constexpr int WAVES = 4;
constexpr int TI = 4;
constexpr int NSPLIT = 4;
constexpr int DT = 1743;
constexpr int OFF = 200;
constexpr int LEAD = 2;
constexpr int PHN = 16;
constexpr int AUX = 2;

constexpr int B_ = 8, HQ = 32, HKV = 8, SK = 8192, D = 128, G = 4;
constexpr int NTHR = WAVES * 64;
constexpr int TKEYS = TI * 4;
constexpr int NT = SK / TKEYS;
constexpr int NW = NSPLIT * WAVES;
constexpr int NS = NT / NW;
constexpr int TBYTES = TKEYS * D * 2;
constexpr float C2 = 0.088388347648318440f * 1.4426950408889634f;
static_assert(NS % 2 == 0, "geometry");

using u32x4 = __attribute__((ext_vector_type(4))) unsigned;
using u32x2 = __attribute__((ext_vector_type(2))) unsigned;
using f32x4 = __attribute__((ext_vector_type(4))) float;
using bf16x8 = __attribute__((ext_vector_type(8))) __bf16;
using s16x4 = __attribute__((ext_vector_type(4))) short;

template <int CTRL> __device__ __forceinline__ float dpp(float x) {
  return __builtin_bit_cast(float, __builtin_amdgcn_mov_dpp(__builtin_bit_cast(int, x), CTRL, 0xf, 0xf, true));
}
__device__ __forceinline__ float xrow_max(float x) {
  auto s = __builtin_amdgcn_permlane16_swap(__float_as_uint(x), __float_as_uint(x), false, false);
  x = fmaxf(__uint_as_float(s[0]), __uint_as_float(s[1]));
  auto t = __builtin_amdgcn_permlane32_swap(__float_as_uint(x), __float_as_uint(x), false, false);
  return fmaxf(__uint_as_float(t[0]), __uint_as_float(t[1]));
}
__device__ __forceinline__ float xrow_sum(float x) {
  auto s = __builtin_amdgcn_permlane16_swap(__float_as_uint(x), __float_as_uint(x), false, false);
  x = __uint_as_float(s[0]) + __uint_as_float(s[1]);
  auto t = __builtin_amdgcn_permlane32_swap(__float_as_uint(x), __float_as_uint(x), false, false);
  return __uint_as_float(t[0]) + __uint_as_float(t[1]);
}
__device__ __forceinline__ unsigned cvtpk(float lo, float hi) {
  using f32x2 = __attribute__((ext_vector_type(2))) float;
  using bf16x2 = __attribute__((ext_vector_type(2))) __bf16;
  f32x2 f = {lo, hi};
  return __builtin_bit_cast(unsigned, __builtin_convertvector(f, bf16x2));
}

constexpr int HSTR = 136;
constexpr int REC = 4 * HSTR + 8;

__global__ void __launch_bounds__(NTHR, 1) attn_split(const char* q, const char* k, const char* v, float* part) {
  __shared__ __attribute__((aligned(16))) float lds[WAVES * REC];
  const int bid = blockIdx.x;
  const int bh = bid / NSPLIT, sp = bid % NSPLIT;
  const int tid = threadIdx.x, lane = tid & 63;
  const int wave = __builtin_amdgcn_readfirstlane(tid >> 6);
  const int g = lane >> 4, c = lane & 15, x = lane & 3, hh = c >> 2;
  const unsigned TP = (unsigned)__builtin_amdgcn_s_memrealtime();

  const size_t base = (size_t)bh * SK * (D * 2);
  const __amdgpu_buffer_rsrc_t rk = __builtin_amdgcn_make_buffer_rsrc((void*)(k + base), (short)0, SK * D * 2, 0x00020000);
  const __amdgpu_buffer_rsrc_t rv = __builtin_amdgcn_make_buffer_rsrc((void*)(v + base), (short)0, SK * D * 2, 0x00020000);
  const unsigned koff = hh * 256 + g * 64 + x * 16;
  const unsigned voff = lane * 16;

  u32x4 K[2][TI], V[2][TI];
  auto issue = [&](int st, int t) {
    const unsigned to = (unsigned)(t * NW + sp * WAVES + wave) * TBYTES;
#pragma unroll
    for (int i = 0; i < TI; ++i) K[st][i] = __builtin_amdgcn_raw_buffer_load_b128(rk, koff + i * 1024, to, AUX);
#pragma unroll
    for (int i = 0; i < TI; ++i) V[st][i] = __builtin_amdgcn_raw_buffer_load_b128(rv, voff + i * 1024, to, AUX);
    asm volatile("" ::: "memory");
  };
  const u32x4 qf = *reinterpret_cast<const u32x4*>(q + ((size_t)(bh * G + hh) * D + (4 * g + x) * 8) * 2);
  __builtin_amdgcn_sched_barrier(0);
  issue(0, 0);
  __builtin_amdgcn_sched_barrier(0);

  const unsigned mask0 = x == 0 ? 0x0000FFFFu : x == 1 ? 0xFFFF0000u : 0u;
  const unsigned mask1 = x == 2 ? 0x0000FFFFu : x == 3 ? 0xFFFF0000u : 0u;
  float m = -INFINITY, l = 0.f;
  f32x4 a1 = {0.f, 0.f, 0.f, 0.f}, a2 = {0.f, 0.f, 0.f, 0.f};

  auto compute = [&](int st) {
    float s[TI];
#pragma unroll
    for (int i = 0; i < TI; ++i) {
      f32x4 z = {0.f, 0.f, 0.f, 0.f};
      f32x4 cc = __builtin_amdgcn_mfma_f32_16x16x32_bf16(__builtin_bit_cast(bf16x8, K[st][i]), __builtin_bit_cast(bf16x8, qf), z, 0, 0, 0);
      s[i] = (dpp<0x00>(cc[0]) + dpp<0x55>(cc[1])) + (dpp<0xAA>(cc[2]) + dpp<0xFF>(cc[3]));
    }
    float tm = s[0];
#pragma unroll
    for (int i = 1; i < TI; ++i) tm = fmaxf(tm, s[i]);
    tm = xrow_max(tm);
    const float mn = fmaxf(m, tm);
    const float alpha = __builtin_amdgcn_exp2f((m - mn) * C2);
    const float mc = mn * C2;
    m = mn;
    a1 *= alpha; a2 *= alpha; l *= alpha;
#pragma unroll
    for (int i = 0; i < TI; ++i) {
      const float p = __builtin_amdgcn_exp2f(fmaf(s[i], C2, -mc));
      l += p;
      const unsigned pp = cvtpk(p, p);
      u32x2 pb = {pp & mask0, pp & mask1};
      u32x2 vlo = {V[st][i][0], V[st][i][1]}, vhi = {V[st][i][2], V[st][i][3]};
      a1 = __builtin_amdgcn_mfma_f32_16x16x16bf16_1k(__builtin_bit_cast(s16x4, vlo), __builtin_bit_cast(s16x4, pb), a1, 0, 0, 0);
      a2 = __builtin_amdgcn_mfma_f32_16x16x16bf16_1k(__builtin_bit_cast(s16x4, vhi), __builtin_bit_cast(s16x4, pb), a2, 0, 0, 0);
    }
    __builtin_amdgcn_sched_barrier(0);
  };

  const unsigned phase = ((unsigned)(wave * (PHN / WAVES) + (bid >> 3)) % PHN) * DT / PHN;
  auto pace = [&](int j) {
    const unsigned due = TP + (j < LEAD ? 0u : (unsigned)((OFF * 16 + phase + (j - LEAD) * DT) >> 4));
#pragma unroll 1
    for (int n = 0; n < 4096; ++n) {
      if ((int)((unsigned)__builtin_amdgcn_s_memrealtime() - due) >= 0) break;
      __builtin_amdgcn_s_sleep(1);
    }
  };
#pragma unroll 1
  for (int t = 0; t < NS - 2; t += 2) {
#pragma unroll
    for (int u = 0; u < 2; ++u) {
      pace(t + u + 1);
      issue(u ^ 1, t + u + 1);
      __builtin_amdgcn_sched_barrier(0);
      compute(u);
    }
  }
  pace(NS - 1);
  issue(1, NS - 1);
  __builtin_amdgcn_sched_barrier(0);
  compute(0);
  compute(1);

  l = xrow_sum(l);
  float* rec = lds + wave * REC;
#pragma unroll
  for (int i = 0; i < 4; ++i) {
    rec[hh * HSTR + 32 * g + 8 * i + x] = a1[i];
    rec[hh * HSTR + 32 * g + 8 * i + 4 + x] = a2[i];
  }
  if (g == 0 && x == 0) { rec[4 * HSTR + hh] = m * C2; rec[4 * HSTR + 4 + hh] = l; }
  __syncthreads();
  for (int e = tid; e < G * D; e += NTHR) {
    const int h = e >> 7, d = e & 127;
    float M = -INFINITY;
#pragma unroll
    for (int w = 0; w < WAVES; ++w) M = fmaxf(M, lds[w * REC + 4 * HSTR + h]);
    float L = 0.f, o = 0.f;
#pragma unroll
    for (int w = 0; w < WAVES; ++w) {
      const float wt = __builtin_amdgcn_exp2f(lds[w * REC + 4 * HSTR + h] - M);
      L = fmaf(wt, lds[w * REC + 4 * HSTR + 4 + h], L);
      o = fmaf(wt, lds[w * REC + h * HSTR + d], o);
    }
    const int row = bh * G + h;
    float* po = part + (size_t)(row * NSPLIT + sp) * (D + 2);
    po[d] = o;
    if (d == 0) { po[D] = M; po[D + 1] = L; }
  }
}

__global__ void __launch_bounds__(64) attn_combine(const float* part, unsigned* __restrict__ out) {
  const int row = blockIdx.x, lane = threadIdx.x;
  const float* p = part + (size_t)row * NSPLIT * (D + 2);
  float mm[NSPLIT], ll[NSPLIT]; float2 oo[NSPLIT];
#pragma unroll
  for (int s = 0; s < NSPLIT; ++s) {
    const float2 ml = *reinterpret_cast<const float2*>(p + s * (D + 2) + D);
    mm[s] = ml.x; ll[s] = ml.y;
    oo[s] = *reinterpret_cast<const float2*>(p + s * (D + 2) + 2 * lane);
  }
  float M = mm[0];
#pragma unroll
  for (int s = 1; s < NSPLIT; ++s) M = fmaxf(M, mm[s]);
  float L = 0.f, o0 = 0.f, o1 = 0.f;
#pragma unroll
  for (int s = 0; s < NSPLIT; ++s) {
    const float w = __builtin_amdgcn_exp2f(mm[s] - M);
    L = fmaf(w, ll[s], L); o0 = fmaf(w, oo[s].x, o0); o1 = fmaf(w, oo[s].y, o1);
  }
  const float inv = 1.f / L;
  out[row * (D / 2) + lane] = cvtpk(o0 * inv, o1 * inv);
}

extern "C" void kernel_launch(void* const* d_in, const int* in_sizes, int n_in, void* d_out, int out_size,
                              void* d_ws, size_t ws_size, hipStream_t stream) {
  attn_split<<<dim3(B_ * HKV * NSPLIT), dim3(NTHR), 0, stream>>>((const char*)d_in[0], (const char*)d_in[1], (const char*)d_in[2], (float*)d_ws);
  attn_combine<<<dim3(B_ * HQ), dim3(64), 0, stream>>>((const float*)d_ws, (unsigned*)d_out);
}
```

```cpp
#include <hip/hip_runtime.h>
#include <hip/hip_bf16.h>
#include <stdint.h>

constexpr int DT = 1743;
constexpr int OFF = 160;
constexpr int WAVES = 4, TI = 8, NSPLIT = 4, AUX = 2, LEAD = 2;
constexpr int B_ = 8, HQ = 32, HKV = 8, SK = 8192, D = 128, G = 4;
constexpr int NW = NSPLIT * WAVES;
constexpr int NH = SK / (4 * TI * NW);
constexpr int NS = 2 * NH;
constexpr int TENSOR = B_ * HKV * SK * D * 2;
constexpr float C2 = 0.088388347648318440f * 1.4426950408889634f;
constexpr unsigned MA[17] = {0x10, 0xf, 0x4, 0xf6, 0x45, 0x48b2, 0x4044, 0xc012, 0x51d0, 0x10871, 0x1282c, 0x680f, 0x75, 0x90, 0x394, 0x2bc, 0x6d5};
constexpr unsigned MP[10] = {0x30, 0x6, 0xd, 0x26, 0x13, 0x2, 0x1f, 0x3f, 0x24, 0x7};
constexpr int SPB0 = 0, SPB1 = 7;

using u32x4 = __attribute__((ext_vector_type(4))) unsigned;
using u32x2 = __attribute__((ext_vector_type(2))) unsigned;
using f32x4 = __attribute__((ext_vector_type(4))) float;
using f32x2 = __attribute__((ext_vector_type(2))) float;
using bf16x8 = __attribute__((ext_vector_type(8))) __bf16;
using s16x4 = __attribute__((ext_vector_type(4))) short;

template <int CTRL> __device__ __forceinline__ unsigned dppu(unsigned x) {
  return (unsigned)__builtin_amdgcn_mov_dpp((int)x, CTRL, 0xf, 0xf, true);
}
template <int CTRL> __device__ __forceinline__ float dpp(float x) { return __uint_as_float(dppu<CTRL>(__float_as_uint(x))); }
__device__ __forceinline__ float xrow_max(float x) {
  auto s = __builtin_amdgcn_permlane16_swap(__float_as_uint(x), __float_as_uint(x), false, false);
  x = fmaxf(__uint_as_float(s[0]), __uint_as_float(s[1]));
  auto t = __builtin_amdgcn_permlane32_swap(__float_as_uint(x), __float_as_uint(x), false, false);
  return fmaxf(__uint_as_float(t[0]), __uint_as_float(t[1]));
}
__device__ __forceinline__ float xrow_sum(float x) {
  auto s = __builtin_amdgcn_permlane16_swap(__float_as_uint(x), __float_as_uint(x), false, false);
  x = __uint_as_float(s[0]) + __uint_as_float(s[1]);
  auto t = __builtin_amdgcn_permlane32_swap(__float_as_uint(x), __float_as_uint(x), false, false);
  return __uint_as_float(t[0]) + __uint_as_float(t[1]);
}
__device__ __forceinline__ unsigned cvtpk(float lo, float hi) {
  using bf16x2 = __attribute__((ext_vector_type(2))) __bf16;
  f32x2 f = {lo, hi};
  return __builtin_bit_cast(unsigned, __builtin_convertvector(f, bf16x2));
}
__device__ __forceinline__ unsigned rt() { return (unsigned)__builtin_amdgcn_s_memrealtime(); }

constexpr int HSTR = 136;
constexpr int REC = 4 * HSTR + 8;
constexpr int SCW = NH * 2 * 64;

__global__ void __launch_bounds__(WAVES * 64, 1) attn_main(const char* q, const char* k, const char* v, float* part) {
  __shared__ __attribute__((aligned(16))) float lds[WAVES * REC];
  __shared__ float sc[WAVES * SCW];
  const unsigned TP = rt();
  const int bid = blockIdx.x;
  const int tid = threadIdx.x, lane = tid & 63;
  const int wave = __builtin_amdgcn_readfirstlane(tid >> 6);
  unsigned base = 0, ph = 0;
#pragma unroll
  for (int i = 0; i < 8; ++i) { base ^= (bid >> i & 1) ? MA[5 + i] : 0u; ph ^= (bid >> i & 1) ? MP[2 + i] : 0u; }
  const int bh = base >> 11, sp = (bid >> SPB0 & 1) | (bid >> SPB1 & 1) << 1;
#pragma unroll
  for (int i = 0; i < 2; ++i) { base ^= (wave >> i & 1) ? MA[3 + i] : 0u; ph ^= (wave >> i & 1) ? MP[i] : 0u; }

  const int g = lane >> 4, c = lane & 15, x = lane & 3, hh = c >> 2;
  const __amdgpu_buffer_rsrc_t rk = __builtin_amdgcn_make_buffer_rsrc((void*)k, (short)0, TENSOR, 0x00020000);
  const __amdgpu_buffer_rsrc_t rv = __builtin_amdgcn_make_buffer_rsrc((void*)v, (short)0, TENSOR, 0x00020000);
  const unsigned koff = hh * 256 + g * 64 + x * 16;
  const unsigned voff = lane * 16;

  u32x4 T[2][TI];
  auto issue = [&](int st, const __amdgpu_buffer_rsrc_t r, unsigned off, int j) {
    unsigned a = base;
#pragma unroll
    for (int i = 0; i < 4; ++i) a ^= (j >> i & 1) ? MA[13 + i] : 0u;
    a <<= 10;
#pragma unroll
    for (int i = 0; i < TI; ++i)
      T[st][i] = __builtin_amdgcn_raw_buffer_load_b128(r, off, a ^ (((i & 1 ? MA[0] : 0u) ^ (i & 2 ? MA[1] : 0u) ^ (i & 4 ? MA[2] : 0u)) << 10), AUX);
    asm volatile("" ::: "memory");
    __builtin_amdgcn_sched_barrier(0);
  };
  const u32x4 qf = *reinterpret_cast<const u32x4*>(q + ((size_t)(bh * G + hh) * D + (4 * g + x) * 8) * 2);
  __builtin_amdgcn_sched_barrier(0);
  issue(0, rk, koff, 0);

  const unsigned mask0 = x == 0 ? 0x0000FFFFu : x == 1 ? 0xFFFF0000u : 0u;
  const unsigned mask1 = x == 2 ? 0x0000FFFFu : x == 3 ? 0xFFFF0000u : 0u;
  float m = -INFINITY, l = 0.f, mc;
  f32x4 a1 = {0.f, 0.f, 0.f, 0.f}, a2 = {0.f, 0.f, 0.f, 0.f};
  float* sl = sc + wave * SCW + lane;

  auto scores = [&](int st, int j) {
#pragma unroll
    for (int n = 0; n < 2; ++n) {
      float s[4];
#pragma unroll
      for (int a = 0; a < 4; ++a) {
        f32x4 z = {0.f, 0.f, 0.f, 0.f};
        f32x4 cc = __builtin_amdgcn_mfma_f32_16x16x32_bf16(__builtin_bit_cast(bf16x8, T[st][4 * n + a]), __builtin_bit_cast(bf16x8, qf), z, 0, 0, 0);
        s[a] = (dpp<0x00>(cc[0]) + dpp<0x55>(cc[1])) + (dpp<0xAA>(cc[2]) + dpp<0xFF>(cc[3]));
      }
      const float sel = (x & 2) ? ((x & 1) ? s[3] : s[2]) : ((x & 1) ? s[1] : s[0]);
      m = fmaxf(m, sel);
      sl[(2 * j + n) * 64] = sel;
    }
    __builtin_amdgcn_sched_barrier(0);
  };
  auto values = [&](int st, int j) {
#pragma unroll
    for (int n = 0; n < 2; ++n) {
      const float p = __builtin_amdgcn_exp2f(fmaf(sl[(2 * j + n) * 64], C2, -mc));
      l += p;
      const unsigned pp = cvtpk(p, p);
      const unsigned pa[4] = {dppu<0x00>(pp), dppu<0x55>(pp), dppu<0xAA>(pp), dppu<0xFF>(pp)};
#pragma unroll
      for (int a = 0; a < 4; ++a) {
        u32x2 pb = {pa[a] & mask0, pa[a] & mask1};
        const u32x4 t = T[st][4 * n + a];
        u32x2 vlo = {t[0], t[1]}, vhi = {t[2], t[3]};
        a1 = __builtin_amdgcn_mfma_f32_16x16x16bf16_1k(__builtin_bit_cast(s16x4, vlo), __builtin_bit_cast(s16x4, pb), a1, 0, 0, 0);
        a2 = __builtin_amdgcn_mfma_f32_16x16x16bf16_1k(__builtin_bit_cast(s16x4, vhi), __builtin_bit_cast(s16x4, pb), a2, 0, 0, 0);
      }
    }
    __builtin_amdgcn_sched_barrier(0);
  };

  const unsigned phase = ph * DT >> 6;
  auto pace = [&](int j) {
    const unsigned due = TP + (j < LEAD ? 0u : (unsigned)((OFF * 16 + phase + (j - LEAD) * DT) >> 4));
#pragma unroll 1
    for (int n = 0; n < 4096; ++n) {
      if ((int)(rt() - due) >= 0) break;
      __builtin_amdgcn_s_sleep(1);
    }
  };
#pragma unroll 1
  for (int t = 0; t < NH - 2; t += 2) {
    pace(t + 1); issue(1, rk, koff, t + 1); scores(0, t);
    pace(t + 2); issue(0, rk, koff, t + 2); scores(1, t + 1);
  }
  pace(NH - 1); issue(1, rk, koff, NH - 1); scores(0, NH - 2);
  pace(NH); issue(0, rv, voff, 0); scores(1, NH - 1);
  m = fmaxf(m, dpp<0xB1>(m)); m = fmaxf(m, dpp<0x4E>(m));
  m = xrow_max(m);
  mc = m * C2;
#pragma unroll 1
  for (int t = 0; t < NH - 2; t += 2) {
    pace(NH + t + 1); issue(1, rv, voff, t + 1); values(0, t);
    pace(NH + t + 2); issue(0, rv, voff, t + 2); values(1, t + 1);
  }
  pace(NS - 1); issue(1, rv, voff, NH - 1); values(0, NH - 2);
  values(1, NH - 1);

  l += dpp<0xB1>(l); l += dpp<0x4E>(l);
  l = xrow_sum(l);
  float* rec = lds + wave * REC;
#pragma unroll
  for (int i = 0; i < 4; ++i) {
    rec[hh * HSTR + 32 * g + 8 * i + x] = a1[i];
    rec[hh * HSTR + 32 * g + 8 * i + 4 + x] = a2[i];
  }
  if (g == 0 && x == 0) *reinterpret_cast<f32x2*>(rec + 4 * HSTR + 2 * hh) = f32x2{mc, l};
  __syncthreads();
  f32x2 ml[WAVES], oo[WAVES];
#pragma unroll
  for (int w = 0; w < WAVES; ++w) {
    ml[w] = *reinterpret_cast<const f32x2*>(lds + w * REC + 4 * HSTR + 2 * wave);
    oo[w] = *reinterpret_cast<const f32x2*>(lds + w * REC + wave * HSTR + 2 * lane);
  }
  const float M = fmaxf(fmaxf(ml[0][0], ml[1][0]), fmaxf(ml[2][0], ml[3][0]));
  float L = 0.f, o0 = 0.f, o1 = 0.f;
#pragma unroll
  for (int w = 0; w < WAVES; ++w) {
    const float wt = __builtin_amdgcn_exp2f(ml[w][0] - M);
    L = fmaf(wt, ml[w][1], L); o0 = fmaf(wt, oo[w][0], o0); o1 = fmaf(wt, oo[w][1], o1);
  }
  float* po = part + (size_t)((bh * G + wave) * NSPLIT + sp) * (D + 2);
  *reinterpret_cast<f32x2*>(po + 2 * lane) = f32x2{o0, o1};
  if (lane == 0) *reinterpret_cast<f32x2*>(po + D) = f32x2{M, L};
}

__global__ void __launch_bounds__(64) attn_combine(const float* part, unsigned* __restrict__ out) {
  const int row = blockIdx.x, lane = threadIdx.x;
  const float* p = part + (size_t)row * NSPLIT * (D + 2);
  float mm[NSPLIT], ll[NSPLIT]; float2 oo[NSPLIT];
#pragma unroll
  for (int s = 0; s < NSPLIT; ++s) {
    const float2 ml = *reinterpret_cast<const float2*>(p + s * (D + 2) + D);
    mm[s] = ml.x; ll[s] = ml.y;
    oo[s] = *reinterpret_cast<const float2*>(p + s * (D + 2) + 2 * lane);
  }
  float M = mm[0];
#pragma unroll
  for (int s = 1; s < NSPLIT; ++s) M = fmaxf(M, mm[s]);
  float L = 0.f, o0 = 0.f, o1 = 0.f;
#pragma unroll
  for (int s = 0; s < NSPLIT; ++s) {
    const float w = __builtin_amdgcn_exp2f(mm[s] - M);
    L = fmaf(w, ll[s], L); o0 = fmaf(w, oo[s].x, o0); o1 = fmaf(w, oo[s].y, o1);
  }
  const float inv = 1.f / L;
  out[row * (D / 2) + lane] = cvtpk(o0 * inv, o1 * inv);
}

extern "C" void kernel_launch(void* const* d_in, const int* in_sizes, int n_in, void* d_out, int out_size,
                              void* d_ws, size_t ws_size, hipStream_t stream) {
  attn_main<<<dim3(B_ * HKV * NSPLIT), dim3(WAVES * 64), 0, stream>>>((const char*)d_in[0], (const char*)d_in[1], (const char*)d_in[2], (float*)d_ws);
  attn_combine<<<dim3(B_ * HQ), dim3(64), 0, stream>>>((const float*)d_ws, (unsigned*)d_out);
}
```

```cpp
#include <hip/hip_runtime.h>
#include <hip/hip_bf16.h>
#include <stdint.h>

constexpr int DT = 1720;
constexpr int OFF = 160;
constexpr int WAVES = 4, TI = 8, NSPLIT = 4, AUX = 2, LEAD = 2;
constexpr int B_ = 8, HQ = 32, HKV = 8, SK = 8192, D = 128, G = 4;
constexpr int NW = NSPLIT * WAVES;
constexpr int NH = SK / (4 * TI * NW);
constexpr int NS = 2 * NH;
constexpr int TENSOR = B_ * HKV * SK * D * 2;
constexpr float C2 = 0.088388347648318440f * 1.4426950408889634f;
constexpr unsigned MA[17] = {0x10, 0xf, 0x4, 0xf6, 0x45, 0x48b2, 0x4044, 0xc012, 0x51d0, 0x10871, 0x1282c, 0x680f, 0x75, 0x90, 0x394, 0x2bc, 0x6d5};
constexpr unsigned MP[10] = {0x30, 0x6, 0xd, 0x26, 0x13, 0x2, 0x1f, 0x3f, 0x24, 0x7};
constexpr int SPB0 = 0, SPB1 = 7;

using u32x4 = __attribute__((ext_vector_type(4))) unsigned;
using u32x2 = __attribute__((ext_vector_type(2))) unsigned;
using f32x4 = __attribute__((ext_vector_type(4))) float;
using f32x2 = __attribute__((ext_vector_type(2))) float;
using bf16x8 = __attribute__((ext_vector_type(8))) __bf16;
using s16x4 = __attribute__((ext_vector_type(4))) short;

template <int CTRL> __device__ __forceinline__ unsigned dppu(unsigned x) {
  return (unsigned)__builtin_amdgcn_mov_dpp((int)x, CTRL, 0xf, 0xf, true);
}
template <int CTRL> __device__ __forceinline__ float dpp(float x) { return __uint_as_float(dppu<CTRL>(__float_as_uint(x))); }
__device__ __forceinline__ float xrow_max(float x) {
  auto s = __builtin_amdgcn_permlane16_swap(__float_as_uint(x), __float_as_uint(x), false, false);
  x = fmaxf(__uint_as_float(s[0]), __uint_as_float(s[1]));
  auto t = __builtin_amdgcn_permlane32_swap(__float_as_uint(x), __float_as_uint(x), false, false);
  return fmaxf(__uint_as_float(t[0]), __uint_as_float(t[1]));
}
__device__ __forceinline__ float xrow_sum(float x) {
  auto s = __builtin_amdgcn_permlane16_swap(__float_as_uint(x), __float_as_uint(x), false, false);
  x = __uint_as_float(s[0]) + __uint_as_float(s[1]);
  auto t = __builtin_amdgcn_permlane32_swap(__float_as_uint(x), __float_as_uint(x), false, false);
  return __uint_as_float(t[0]) + __uint_as_float(t[1]);
}
__device__ __forceinline__ unsigned cvtpk(float lo, float hi) {
  using bf16x2 = __attribute__((ext_vector_type(2))) __bf16;
  f32x2 f = {lo, hi};
  return __builtin_bit_cast(unsigned, __builtin_convertvector(f, bf16x2));
}
__device__ __forceinline__ unsigned rt() { return (unsigned)__builtin_amdgcn_s_memrealtime(); }

constexpr int HSTR = 136;
constexpr int REC = 4 * HSTR + 8;
constexpr int SCW = NH * 2 * 64;
constexpr int PR = 256;

static __device__ __attribute__((aligned(256))) float gpart[B_ * HQ * NSPLIT * PR];

__global__ void __launch_bounds__(WAVES * 64, 1) attn_main(const char* q, const char* k, const char* v) {
  __shared__ __attribute__((aligned(16))) float lds[WAVES * REC];
  __shared__ float sc[WAVES * SCW];
  const int bid = blockIdx.x;
  const int tid = threadIdx.x, lane = tid & 63;
  const int wave = __builtin_amdgcn_readfirstlane(tid >> 6);
  unsigned base = 0, ph = 0;
#pragma unroll
  for (int i = 0; i < 8; ++i) { base ^= (bid >> i & 1) ? MA[5 + i] : 0u; ph ^= (bid >> i & 1) ? MP[2 + i] : 0u; }
  const int bh = base >> 11, sp = (bid >> SPB0 & 1) | (bid >> SPB1 & 1) << 1;
#pragma unroll
  for (int i = 0; i < 2; ++i) { base ^= (wave >> i & 1) ? MA[3 + i] : 0u; ph ^= (wave >> i & 1) ? MP[i] : 0u; }

  const int g = lane >> 4, c = lane & 15, x = lane & 3, hh = c >> 2;
  const __amdgpu_buffer_rsrc_t rk = __builtin_amdgcn_make_buffer_rsrc((void*)k, (short)0, TENSOR, 0x00020000);
  const __amdgpu_buffer_rsrc_t rv = __builtin_amdgcn_make_buffer_rsrc((void*)v, (short)0, TENSOR, 0x00020000);
  const unsigned koff = hh * 256 + g * 64 + x * 16;
  const unsigned voff = lane * 16;

  u32x4 T[2][TI];
  auto issue = [&](int st, const __amdgpu_buffer_rsrc_t r, unsigned off, int j) {
    unsigned a = base;
#pragma unroll
    for (int i = 0; i < 4; ++i) a ^= (j >> i & 1) ? MA[13 + i] : 0u;
    a <<= 10;
#pragma unroll
    for (int i = 0; i < TI; ++i)
      T[st][i] = __builtin_amdgcn_raw_buffer_load_b128(r, off, a ^ (((i & 1 ? MA[0] : 0u) ^ (i & 2 ? MA[1] : 0u) ^ (i & 4 ? MA[2] : 0u)) << 10), AUX);
    asm volatile("" ::: "memory");
    __builtin_amdgcn_sched_barrier(0);
  };
  const u32x4 qf = *reinterpret_cast<const u32x4*>(q + ((size_t)(bh * G + hh) * D + (4 * g + x) * 8) * 2);
  __builtin_amdgcn_sched_barrier(0);
  issue(0, rk, koff, 0);
  const unsigned T0 = rt();
  __builtin_amdgcn_sched_barrier(0);

  const unsigned mask0 = x == 0 ? 0x0000FFFFu : x == 1 ? 0xFFFF0000u : 0u;
  const unsigned mask1 = x == 2 ? 0x0000FFFFu : x == 3 ? 0xFFFF0000u : 0u;
  float m = -INFINITY, l = 0.f, mc;
  f32x4 a1 = {0.f, 0.f, 0.f, 0.f}, a2 = {0.f, 0.f, 0.f, 0.f};
  float* sl = sc + wave * SCW + lane;

  auto scores = [&](int st, int j) {
#pragma unroll
    for (int n = 0; n < 2; ++n) {
      float s[4];
#pragma unroll
      for (int a = 0; a < 4; ++a) {
        f32x4 z = {0.f, 0.f, 0.f, 0.f};
        f32x4 cc = __builtin_amdgcn_mfma_f32_16x16x32_bf16(__builtin_bit_cast(bf16x8, T[st][4 * n + a]), __builtin_bit_cast(bf16x8, qf), z, 0, 0, 0);
        s[a] = (dpp<0x00>(cc[0]) + dpp<0x55>(cc[1])) + (dpp<0xAA>(cc[2]) + dpp<0xFF>(cc[3]));
      }
      const float sel = (x & 2) ? ((x & 1) ? s[3] : s[2]) : ((x & 1) ? s[1] : s[0]);
      m = fmaxf(m, sel);
      sl[(2 * j + n) * 64] = sel;
    }
    __builtin_amdgcn_sched_barrier(0);
  };
  auto values = [&](int st, int j) {
#pragma unroll
    for (int n = 0; n < 2; ++n) {
      const float p = __builtin_amdgcn_exp2f(fmaf(sl[(2 * j + n) * 64], C2, -mc));
      l += p;
      const unsigned pp = cvtpk(p, p);
      const unsigned pa[4] = {dppu<0x00>(pp), dppu<0x55>(pp), dppu<0xAA>(pp), dppu<0xFF>(pp)};
#pragma unroll
      for (int a = 0; a < 4; ++a) {
        u32x2 pb = {pa[a] & mask0, pa[a] & mask1};
        const u32x4 t = T[st][4 * n + a];
        u32x2 vlo = {t[0], t[1]}, vhi = {t[2], t[3]};
        a1 = __builtin_amdgcn_mfma_f32_16x16x16bf16_1k(__builtin_bit_cast(s16x4, vlo), __builtin_bit_cast(s16x4, pb), a1, 0, 0, 0);
        a2 = __builtin_amdgcn_mfma_f32_16x16x16bf16_1k(__builtin_bit_cast(s16x4, vhi), __builtin_bit_cast(s16x4, pb), a2, 0, 0, 0);
      }
    }
    __builtin_amdgcn_sched_barrier(0);
  };

  const unsigned phase = ph * DT >> 6;
  auto pace = [&](int j) {
    const unsigned due = T0 + (j < LEAD ? 0u : (unsigned)((OFF * 16 + phase + (j - LEAD) * DT) >> 4));
#pragma unroll 1
    for (int n = 0; n < 4096; ++n) {
      if ((int)(rt() - due) >= 0) break;
      __builtin_amdgcn_s_sleep(1);
    }
  };
#pragma unroll 1
  for (int t = 0; t < NH - 2; t += 2) {
    pace(t + 1); issue(1, rk, koff, t + 1); scores(0, t);
    pace(t + 2); issue(0, rk, koff, t + 2); scores(1, t + 1);
  }
  pace(NH - 1); issue(1, rk, koff, NH - 1); scores(0, NH - 2);
  pace(NH); issue(0, rv, voff, 0); scores(1, NH - 1);
  m = fmaxf(m, dpp<0xB1>(m)); m = fmaxf(m, dpp<0x4E>(m));
  m = xrow_max(m);
  mc = m * C2;
#pragma unroll 1
  for (int t = 0; t < NH - 2; t += 2) {
    pace(NH + t + 1); issue(1, rv, voff, t + 1); values(0, t);
    pace(NH + t + 2); issue(0, rv, voff, t + 2); values(1, t + 1);
  }
  pace(NS - 1); issue(1, rv, voff, NH - 1); values(0, NH - 2);
  values(1, NH - 1);

  l += dpp<0xB1>(l); l += dpp<0x4E>(l);
  l = xrow_sum(l);
  float* rec = lds + wave * REC;
#pragma unroll
  for (int i = 0; i < 4; ++i) {
    rec[hh * HSTR + 32 * g + 8 * i + x] = a1[i];
    rec[hh * HSTR + 32 * g + 8 * i + 4 + x] = a2[i];
  }
  if (g == 0 && x == 0) *reinterpret_cast<f32x2*>(rec + 4 * HSTR + 2 * hh) = f32x2{mc, l};
  __syncthreads();
  f32x2 ml[WAVES], oo[WAVES];
#pragma unroll
  for (int w = 0; w < WAVES; ++w) {
    ml[w] = *reinterpret_cast<const f32x2*>(lds + w * REC + 4 * HSTR + 2 * wave);
    oo[w] = *reinterpret_cast<const f32x2*>(lds + w * REC + wave * HSTR + 2 * lane);
  }
  const float M = fmaxf(fmaxf(ml[0][0], ml[1][0]), fmaxf(ml[2][0], ml[3][0]));
  float L = 0.f, o0 = 0.f, o1 = 0.f;
#pragma unroll
  for (int w = 0; w < WAVES; ++w) {
    const float wt = __builtin_amdgcn_exp2f(ml[w][0] - M);
    L = fmaf(wt, ml[w][1], L); o0 = fmaf(wt, oo[w][0], o0); o1 = fmaf(wt, oo[w][1], o1);
  }
  const __amdgpu_buffer_rsrc_t rp = __builtin_amdgcn_make_buffer_rsrc((void*)gpart, (short)0, B_ * HQ * NSPLIT * PR * 4, 0x00020000);
  const unsigned po = ((bh * G + wave) * NSPLIT + sp) * PR * 4;
  __builtin_amdgcn_raw_buffer_store_b128(u32x4{__float_as_uint(o0), __float_as_uint(o1), __float_as_uint(M), __float_as_uint(L)}, rp, lane * 16, po, 0);
}

__global__ void __launch_bounds__(64) attn_combine(unsigned* out) {
  using gf4 = __attribute__((address_space(1))) const f32x4*;
  using gu = __attribute__((address_space(1))) unsigned*;
  const char* pb = (const char*)gpart;
  asm("" : "+s"(pb));
  const unsigned vo = blockIdx.x * (NSPLIT * PR * 4) + threadIdx.x * 16;
  f32x4 r[NSPLIT];
#pragma unroll
  for (int s = 0; s < NSPLIT; ++s) r[s] = *(gf4)(pb + vo + s * (PR * 4));
  const float M = fmaxf(fmaxf(r[0][2], r[1][2]), fmaxf(r[2][2], r[3][2]));
  float L = 0.f, o0 = 0.f, o1 = 0.f;
#pragma unroll
  for (int s = 0; s < NSPLIT; ++s) {
    const float w = __builtin_amdgcn_exp2f(r[s][2] - M);
    L = fmaf(w, r[s][3], L); o0 = fmaf(w, r[s][0], o0); o1 = fmaf(w, r[s][1], o1);
  }
  const float inv = __builtin_amdgcn_rcpf(L);
  const unsigned oo = blockIdx.x * 256 + threadIdx.x * 4;
  *(gu)((char*)out + oo) = cvtpk(o0 * inv, o1 * inv);
}

extern "C" void kernel_launch(void* const* d_in, const int* in_sizes, int n_in, void* d_out, int out_size,
                              void* d_ws, size_t ws_size, hipStream_t stream) {
  attn_main<<<dim3(B_ * HKV * NSPLIT), dim3(WAVES * 64), 0, stream>>>((const char*)d_in[0], (const char*)d_in[1], (const char*)d_in[2]);
  attn_combine<<<dim3(B_ * HQ), dim3(64), 0, stream>>>((unsigned*)d_out);
}
```
